```python
import torch

def _softmax(x, axis=-1):
    return torch.softmax(x, dim=axis)
def _gelu(x):
    return torch.nn.functional.gelu(x, approximate="tanh")
def _silu(x):
    return torch.nn.functional.silu(x)
def _sigmoid(x):
    return torch.sigmoid(x)
def _layernorm(x, axis=-1, eps=1e-5):
    m = x.mean(dim=axis, keepdim=True)
    v = x.var(dim=axis, keepdim=True, unbiased=False)
    return (x - m) / torch.sqrt(v + eps)
def _scatter_add(tgt, idx, src, axis=0):
    return tgt.clone().index_add_(axis, idx, src)
def _segment_sum(src, seg, n):
    out = torch.zeros((n,) + src.shape[1:], dtype=src.dtype)
    return out.index_add_(0, seg, src)
def _pad(x, pw):
    flat = tuple(v for pair in reversed(pw) for v in pair)
    return torch.nn.functional.pad(x, flat)

kernel_name = 'recurrent_scan__sliding_block_cached__swiglu_ffn__decode'

def setup_inputs():

    _w = (lambda a: (lambda t: t.to(torch.bfloat16) if t.is_floating_point() else t)(torch.as_tensor(a)))
    def _A_setup():
        B, Sq, D = 1, 1, 576
        Hq, Hkv, Sk, Dh = 32, 16, 16384, 128
        assert Hq % Hkv == 0, (Hq, Hkv)
        return {
            "x": _w((lambda *s: torch.randn(*s, dtype=torch.bfloat16) * 0.05)(B, Sq, D)),
            "k_cache": _w((lambda *s: torch.randn(*s, dtype=torch.bfloat16) * 1.6)(B, Hkv, Sk, Dh)),
            "v_cache": _w((lambda *s: torch.randn(*s, dtype=torch.bfloat16) * 0.2)(B, Hkv, Sk, Dh)),
            "wq": _w((lambda *s: torch.randn(*s, dtype=torch.bfloat16) * 1.6)(D, Hq * Dh) * (D ** -0.5)),
            "wo": _w((lambda *s: torch.randn(*s, dtype=torch.bfloat16) * 0.2)(Hq * Dh, D) * ((Hq * Dh) ** -0.5)),
        }
    _A_in = _A_setup()

    def _B_setup():
        B, S, D, F = 1, 1, 576, 4608
        return {
            "x": _w((lambda *s: torch.randn(*s, dtype=torch.bfloat16) * 0.05)(B, S, D)),
            "w_gate": _w((lambda *s: torch.randn(*s, dtype=torch.bfloat16) * 0.1)(D, F)),
            "w_up": _w((lambda *s: torch.randn(*s, dtype=torch.bfloat16) * 0.1)(D, F)),
            "w_down": _w((lambda *s: torch.randn(*s, dtype=torch.bfloat16) * 0.1)(F, D)),
        }
    _B_in = _B_setup()

    return {
        "A_x": _A_in["x"],
        "A_k_cache": _A_in["k_cache"],
        "A_v_cache": _A_in["v_cache"],
        "A_wq": _A_in["wq"],
        "A_wo": _A_in["wo"],
        "B_x": _B_in["x"],
        "B_w_gate": _B_in["w_gate"],
        "B_w_up": _B_in["w_up"],
        "B_w_down": _B_in["w_down"],
    }

def reference(A_x, A_k_cache, A_v_cache, A_wq, A_wo, B_x, B_w_gate, B_w_up, B_w_down):
    def _A_ref(x, k_cache, v_cache, wq, wo):
        B, Sq, D = x.shape
        Hq, Dh = 32, 128
        Hkv, Sk = k_cache.shape[1], k_cache.shape[2]
        W = min(4096, max(256, Sk // 4))
        rep = Hq // Hkv
        q = (lambda x, *d: x.permute(*d))((x @ wq).reshape(B, Sq, Hq, Dh), 0, 2, 1, 3)
        k = (lambda x, n, axis: torch.repeat_interleave(x, n, dim=axis))(k_cache[:, :, Sk - W:, :], rep, axis=1)
        v = (lambda x, n, axis: torch.repeat_interleave(x, n, dim=axis))(v_cache[:, :, Sk - W:, :], rep, axis=1)
        scores = torch.einsum("bhqd,bhkd->bhqk", q, k) * (Dh ** -0.5)
        p = _softmax(scores, axis=-1)
        o = torch.einsum("bhqk,bhkd->bhqd", p, v)
        return (lambda x, *d: x.permute(*d))(o, 0, 2, 1, 3).reshape(B, Sq, Hq * Dh) @ wo

    def _B_ref(x, w_gate, w_up, w_down):
        g = _silu(x @ w_gate)
        u = x @ w_up
        return (g * u) @ w_down

    x = A_x
    xn = _layernorm(x, axis=-1)
    s = _A_ref(xn, A_k_cache, A_v_cache, A_wq, A_wo)
    g = _silu(_B_ref(xn, B_w_gate, B_w_up, B_w_down))
    return x + s * g
```

```cpp
#include <hip/hip_runtime.h>
#include <hip/hip_bf16.h>
#include <stdint.h>

typedef unsigned int u32;
typedef unsigned long long u64;
typedef unsigned short u16;
typedef __attribute__((ext_vector_type(4))) unsigned u32x4;
typedef __attribute__((ext_vector_type(4))) float f32x4;
typedef __attribute__((ext_vector_type(2))) __bf16 bf16x2;
#define GAS __attribute__((address_space(1)))
typedef GAS u64 gu64;
typedef GAS u32 gu32;
#define RLX_AGENT __ATOMIC_RELAXED, __HIP_MEMORY_SCOPE_AGENT

namespace {

constexpr int D = 576, HKV = 16, SK = 16384, DH = 128, WIN = 4096, FF = 4608, QC = 4096;
constexpr int NWG = 256, NTHR = 512;
constexpr int NTILE_A = 208;
constexpr int NSPLIT = 16;
constexpr int OREC = 136;
constexpr int NFIN = 72;

constexpr size_t WS_CNT = 0;
constexpr size_t WS_GQ = 32;
constexpr size_t WS_GG = WS_GQ + QC / 2;
constexpr size_t WS_GU = WS_GG + FF / 2;
constexpr size_t WS_GO = WS_GU + FF / 2;
constexpr size_t WS_GP = WS_GO + 32 * NSPLIT * OREC;
constexpr size_t WS_END = WS_GP + (size_t)NWG * D;

constexpr unsigned SPIN_CAP = 1u << 20;

__device__ __forceinline__ float bf2f(u16 v) { return __builtin_bit_cast(float, (u32)v << 16); }
__device__ __forceinline__ float bflo(u32 v) { return __builtin_bit_cast(float, v << 16); }
__device__ __forceinline__ float bfhi(u32 v) { return __builtin_bit_cast(float, v & 0xffff0000u); }
__device__ __forceinline__ u32 f2bf(float f) {
  u32 u = __builtin_bit_cast(u32, f);
  return (u + 0x7fffu + ((u >> 16) & 1u)) >> 16;
}
__device__ __forceinline__ float bfr(float f) { return __builtin_bit_cast(float, f2bf(f) << 16); }
__device__ __forceinline__ float dot2(u32 a, u32 b, float acc) {
  return __builtin_amdgcn_fdot2_f32_bf16(__builtin_bit_cast(bf16x2, a), __builtin_bit_cast(bf16x2, b), acc, false);
}
template <int CTRL> __device__ __forceinline__ float dpp(float x) {
  return __builtin_bit_cast(float, __builtin_amdgcn_mov_dpp(__builtin_bit_cast(int, x), CTRL, 0xf, 0xf, true));
}
constexpr int XOR1 = 0xB1, XOR2 = 0x4E, XOR7 = 0x141, XOR8 = 0x128, ROR4 = 0x124;
__device__ __forceinline__ float xrow_max(float x) {
  auto s = __builtin_amdgcn_permlane16_swap(__float_as_uint(x), __float_as_uint(x), false, false);
  x = fmaxf(__uint_as_float(s[0]), __uint_as_float(s[1]));
  auto t = __builtin_amdgcn_permlane32_swap(__float_as_uint(x), __float_as_uint(x), false, false);
  return fmaxf(__uint_as_float(t[0]), __uint_as_float(t[1]));
}
__device__ __forceinline__ float xrow_sum(float x) {
  auto s = __builtin_amdgcn_permlane16_swap(__float_as_uint(x), __float_as_uint(x), false, false);
  x = __uint_as_float(s[0]) + __uint_as_float(s[1]);
  auto t = __builtin_amdgcn_permlane32_swap(__float_as_uint(x), __float_as_uint(x), false, false);
  return __uint_as_float(t[0]) + __uint_as_float(t[1]);
}
__device__ __forceinline__ float wave_sum(float x) {
  x += dpp<XOR1>(x); x += dpp<XOR2>(x); x += dpp<ROR4>(x); x += dpp<XOR8>(x);
  return xrow_sum(x);
}
__device__ __forceinline__ void st_gran(gu64* p, u32 tag, u32 val) {
  __hip_atomic_store(p, ((u64)tag << 32) | (u64)val, RLX_AGENT);
}
__device__ __forceinline__ u64 ld_gran(gu64* p) { return __hip_atomic_load(p, RLX_AGENT); }
__device__ __forceinline__ u32x4 ld_stream(const void* p) { return __builtin_nontemporal_load(reinterpret_cast<const u32x4*>(p)); }
__device__ __forceinline__ float silu_f(float v) { return v / (1.0f + __expf(-v)); }

constexpr int L_PART = 0;
constexpr int L_QS = 512;
constexpr int L_P = 640;
constexpr int L_MRG = L_P + 512;
constexpr int L_OS = L_MRG + 8 * 2 * 132;
constexpr int L_VAL = L_OS + 16 * 36;
constexpr int L_RED = L_VAL + 40;
constexpr int L_FIN = L_RED + 7 * 576;
constexpr int L_TOTAL = L_FIN + 128;

__global__ void __launch_bounds__(NTHR, 2)
fused_block_kernel(const u16* __restrict__ x, const u16* __restrict__ kc, const u16* __restrict__ vc,
                   const u16* __restrict__ wq, const u16* __restrict__ wo, const u16* __restrict__ wg,
                   const u16* __restrict__ wu, const u16* __restrict__ wd, u16* __restrict__ out,
                   u64* wsraw) {
  __shared__ __attribute__((aligned(16))) float lds[L_TOTAL];
  gu64* ws = (gu64*)wsraw;
  const int b = blockIdx.x, tid = threadIdx.x, lane = tid & 63;
  const int wave = __builtin_amdgcn_readfirstlane(tid >> 6);

  const u32 epoch = (u32)__hip_atomic_load((gu32*)(ws + WS_CNT), RLX_AGENT) + 1u;

  float xs[9];
#pragma unroll
  for (int i = 0; i < 9; ++i) xs[i] = bf2f(x[lane + 64 * i]);
  const bool hasA = b < NTILE_A;
  float xr[9];
#pragma unroll
  for (int i = 0; i < 9; ++i) xr[i] = bf2f(x[72 * wave + 8 * i + (lane >> 3)]);

  const u16* wa; int ncolsA, col0; size_t dstA;
  if (b < 64) { wa = wq; ncolsA = QC; col0 = 64 * b; dstA = WS_GQ + 32 * b; }
  else if (b < 136) { wa = wg; ncolsA = FF; col0 = 64 * (b - 64); dstA = WS_GG + 32 * (b - 64); }
  else { wa = wu; ncolsA = FF; col0 = 64 * (b - 136); dstA = WS_GU + 32 * (b - 136); }
  u32x4 wA[9];
  if (hasA) {
    const u16* pa = wa + (size_t)(72 * wave + (lane >> 3)) * ncolsA + col0 + 8 * (lane & 7);
#pragma unroll
    for (int i = 0; i < 9; ++i) wA[i] = ld_stream(pa + (size_t)(8 * i) * ncolsA);
  }

  const int kvh = b >> 4, spl = b & 15;
  const size_t kvoff = ((size_t)kvh * SK + (SK - WIN) + 256 * spl + 32 * wave) * DH + 8 * lane;
  u32x4 Kr[8], Vr[8];
#pragma unroll
  for (int i = 0; i < 8; ++i) Kr[i] = ld_stream(kc + kvoff + 512 * i);
#pragma unroll
  for (int i = 0; i < 8; ++i) Vr[i] = ld_stream(vc + kvoff + 512 * i);

  const bool isWo = b < 128;
  const int nrowsC = isWo ? 32 : 36;
  const u16* wc = isWo ? (wo + (size_t)(32 * b) * D) : (wd + (size_t)(36 * (b - 128)) * D);
  const int r0 = tid / 72, cc = tid - 72 * r0;
  u32x4 wC[6];
#pragma unroll
  for (int i = 0; i < 6; ++i) {
    const int row = 7 * i + r0;
    wC[i] = u32x4{0u, 0u, 0u, 0u};
    if (tid < 504 && row < nrowsC) wC[i] = ld_stream(wc + (size_t)(504 * i + tid) * 8);
  }

  float sum = 0.f;
#pragma unroll
  for (int i = 0; i < 9; ++i) sum += xs[i];
  const float mean = wave_sum(sum) * (1.0f / D);
  float sq = 0.f;
#pragma unroll
  for (int i = 0; i < 9; ++i) { const float d = xs[i] - mean; sq += d * d; }
  const float var = wave_sum(sq) * (1.0f / D);
  const float m_bf = bfr(mean), v_bf = bfr(var);
  const float den = bfr(sqrtf(bfr(v_bf + 1e-5f)));

  if (hasA) {
    float acc[8];
#pragma unroll
    for (int k = 0; k < 8; ++k) acc[k] = 0.f;
#pragma unroll
    for (int i = 0; i < 9; ++i) {
      const float xn = bfr(bfr(xr[i] - m_bf) / den);
#pragma unroll
      for (int k = 0; k < 4; ++k) { acc[2 * k] = fmaf(xn, bflo(wA[i][k]), acc[2 * k]); acc[2 * k + 1] = fmaf(xn, bfhi(wA[i][k]), acc[2 * k + 1]); }
    }
#pragma unroll
    for (int k = 0; k < 8; ++k) { acc[k] += dpp<XOR8>(acc[k]); acc[k] = xrow_sum(acc[k]); }
    if (lane < 8) {
      *reinterpret_cast<f32x4*>(&lds[L_PART + 64 * wave + 8 * lane]) = f32x4{acc[0], acc[1], acc[2], acc[3]};
      *reinterpret_cast<f32x4*>(&lds[L_PART + 64 * wave + 8 * lane + 4]) = f32x4{acc[4], acc[5], acc[6], acc[7]};
    }
  }
  __syncthreads();
  if (hasA && tid < 32) {
    float s0 = 0.f, s1 = 0.f;
#pragma unroll
    for (int w = 0; w < 8; ++w) { s0 += lds[L_PART + 64 * w + 2 * tid]; s1 += lds[L_PART + 64 * w + 2 * tid + 1]; }
    st_gran(ws + dstA + tid, epoch, f2bf(s0) | (f2bf(s1) << 16));
  }

  if (wave == 0) {
    gu64* g = ws + WS_GQ + 128 * kvh + lane;
    u64 a0, a1;
    for (unsigned spins = 0;; ++spins) {
      a0 = ld_gran(g); a1 = ld_gran(g + 64);
      const bool ok = (u32)(a0 >> 32) == epoch && (u32)(a1 >> 32) == epoch;
      if (__all(ok) || spins > SPIN_CAP) break;
      __builtin_amdgcn_s_sleep(1);
    }
    reinterpret_cast<u32*>(lds + L_QS)[lane] = (u32)a0;
    reinterpret_cast<u32*>(lds + L_QS)[64 + lane] = (u32)a1;
  }
  __syncthreads();

  {
    const int r = lane >> 4, c = lane & 15;
    const u32x4 q0 = *reinterpret_cast<const u32x4*>(lds + L_QS + 4 * c);
    const u32x4 q1 = *reinterpret_cast<const u32x4*>(lds + L_QS + 64 + 4 * c);
    float xv[16];
#pragma unroll
    for (int i = 0; i < 8; ++i) {
      float d0 = 0.f, d1 = 0.f;
#pragma unroll
      for (int k = 0; k < 4; ++k) { d0 = dot2(Kr[i][k], q0[k], d0); d1 = dot2(Kr[i][k], q1[k], d1); }
      xv[2 * i] = d0; xv[2 * i + 1] = d1;
    }
#define TR_STEP(HALF, CTRL, BIT) _Pragma("unroll") for (int h = 0; h < (HALF); ++h) { \
      const float lo_ = xv[h], hi_ = xv[h + (HALF)]; const float t1_ = lo_ + dpp<CTRL>(lo_), t2_ = hi_ + dpp<CTRL>(hi_); \
      xv[h] = (c & (BIT)) ? t2_ : t1_; }
    TR_STEP(8, XOR8, 8) TR_STEP(4, XOR7, 4) TR_STEP(2, XOR2, 2) TR_STEP(1, XOR1, 1)
#undef TR_STEP
    const float sc = bfr(bfr(xv[0]) * 0.08838834764831845f);
    float mx = sc;
    mx = fmaxf(mx, dpp<XOR2>(mx)); mx = fmaxf(mx, dpp<ROR4>(mx)); mx = fmaxf(mx, dpp<XOR8>(mx)); mx = xrow_max(mx);
    const float p = __expf(sc - mx);
    float ls = p;
    ls += dpp<XOR2>(ls); ls += dpp<ROR4>(ls); ls += dpp<XOR8>(ls); ls = xrow_sum(ls);
    float* prow = lds + L_P + 64 * wave + 16 * r;
    prow[c] = p;
    asm volatile("s_waitcnt lgkmcnt(0)" ::: "memory");
    f32x4 pr[4];
#pragma unroll
    for (int u = 0; u < 4; ++u) pr[u] = *reinterpret_cast<const f32x4*>(prow + 4 * u);
    float o0[8], o1[8];
#pragma unroll
    for (int k = 0; k < 8; ++k) { o0[k] = 0.f; o1[k] = 0.f; }
#pragma unroll
    for (int i = 0; i < 8; ++i) {
      const float p0 = pr[i >> 1][(2 * i) & 3], p1 = pr[i >> 1][(2 * i + 1) & 3];
#pragma unroll
      for (int k = 0; k < 4; ++k) {
        const float vl = bflo(Vr[i][k]), vh = bfhi(Vr[i][k]);
        o0[2 * k] = fmaf(p0, vl, o0[2 * k]); o0[2 * k + 1] = fmaf(p0, vh, o0[2 * k + 1]);
        o1[2 * k] = fmaf(p1, vl, o1[2 * k]); o1[2 * k + 1] = fmaf(p1, vh, o1[2 * k + 1]);
      }
    }
#pragma unroll
    for (int k = 0; k < 8; ++k) { o0[k] = xrow_sum(o0[k]); o1[k] = xrow_sum(o1[k]); }
    float* rec0 = lds + L_MRG + (wave * 2 + 0) * 132;
    float* rec1 = lds + L_MRG + (wave * 2 + 1) * 132;
    if (r == 0) {
      *reinterpret_cast<f32x4*>(rec0 + 4 + 8 * c) = f32x4{o0[0], o0[1], o0[2], o0[3]};
      *reinterpret_cast<f32x4*>(rec0 + 8 + 8 * c) = f32x4{o0[4], o0[5], o0[6], o0[7]};
      *reinterpret_cast<f32x4*>(rec1 + 4 + 8 * c) = f32x4{o1[0], o1[1], o1[2], o1[3]};
      *reinterpret_cast<f32x4*>(rec1 + 8 + 8 * c) = f32x4{o1[4], o1[5], o1[6], o1[7]};
      if (c < 2) { float* rec = c ? rec1 : rec0; rec[0] = mx; rec[1] = ls; }
    }
  }
  __syncthreads();
  if (tid < 256) {
    const int h = tid >> 7, d = tid & 127;
    float M = -INFINITY;
#pragma unroll
    for (int w = 0; w < 8; ++w) M = fmaxf(M, lds[L_MRG + (w * 2 + h) * 132]);
    float L = 0.f, o = 0.f;
#pragma unroll
    for (int w = 0; w < 8; ++w) {
      const float* rec = lds + L_MRG + (w * 2 + h) * 132;
      const float wt = __expf(rec[0] - M);
      L = fmaf(wt, rec[1], L); o = fmaf(wt, rec[4 + d], o);
    }
    gu64* g = ws + WS_GO + (size_t)((2 * kvh + h) * NSPLIT + spl) * OREC;
    st_gran(g + d, epoch, __builtin_bit_cast(u32, o));
    if (d < 2) st_gran(g + 128 + d, epoch, __builtin_bit_cast(u32, d ? L : M));
  }

  if (isWo) {
    const int h = b >> 2, dq = b & 3;
    const int s = 2 * wave + (lane >> 5);
    gu64* g = ws + WS_GO + (size_t)(h * NSPLIT + s) * OREC;
    u64 a0, a1;
    for (unsigned spins = 0;; ++spins) {
      a0 = ld_gran(g + 32 * dq + (lane & 31));
      a1 = ld_gran(g + 128 + (lane & 1));
      const bool ok = (u32)(a0 >> 32) == epoch && (u32)(a1 >> 32) == epoch;
      if (__all(ok) || spins > SPIN_CAP) break;
      __builtin_amdgcn_s_sleep(1);
    }
    lds[L_OS + 36 * s + (lane & 31)] = __builtin_bit_cast(float, (u32)a0);
    if ((lane & 31) < 2) lds[L_OS + 36 * s + 32 + (lane & 1)] = __builtin_bit_cast(float, (u32)a1);
  } else if (wave == 0) {
    const int cidx = b - 128;
    gu64* g = ws + ((lane < 32) ? WS_GG : WS_GU) + 18 * cidx + (lane & 31);
    const bool act = (lane & 31) < 18;
    u64 a0 = 0;
    for (unsigned spins = 0;; ++spins) {
      if (act) a0 = ld_gran(g);
      const bool ok = !act || (u32)(a0 >> 32) == epoch;
      if (__all(ok) || spins > SPIN_CAP) break;
      __builtin_amdgcn_s_sleep(1);
    }
    const u32 gp = (u32)a0;
    const u32 up = (u32)__shfl((int)gp, lane + 32);
    if (lane < 18) {
      const float g0 = bflo(gp), g1 = bfhi(gp), u0 = bflo(up), u1 = bfhi(up);
      lds[L_VAL + 2 * lane] = bfr(bfr(silu_f(g0)) * u0);
      lds[L_VAL + 2 * lane + 1] = bfr(bfr(silu_f(g1)) * u1);
    }
  }
  __syncthreads();
  if (isWo && tid < 32) {
    float M = -INFINITY;
#pragma unroll
    for (int s = 0; s < NSPLIT; ++s) M = fmaxf(M, lds[L_OS + 36 * s + 32]);
    float L = 0.f, o = 0.f;
#pragma unroll
    for (int s = 0; s < NSPLIT; ++s) {
      const float wt = __expf(lds[L_OS + 36 * s + 32] - M);
      L = fmaf(wt, lds[L_OS + 36 * s + 33], L); o = fmaf(wt, lds[L_OS + 36 * s + tid], o);
    }
    lds[L_VAL + tid] = bfr(o / L);
  }
  __syncthreads();

  {
    float acc[8];
#pragma unroll
    for (int k = 0; k < 8; ++k) acc[k] = 0.f;
#pragma unroll
    for (int i = 0; i < 6; ++i) {
      const int row = 7 * i + r0;
      if (tid < 504 && row < nrowsC) {
        const float v = lds[L_VAL + row];
#pragma unroll
        for (int k = 0; k < 4; ++k) { acc[2 * k] = fmaf(v, bflo(wC[i][k]), acc[2 * k]); acc[2 * k + 1] = fmaf(v, bfhi(wC[i][k]), acc[2 * k + 1]); }
      }
    }
    if (tid < 504) {
      *reinterpret_cast<f32x4*>(&lds[L_RED + 576 * r0 + 8 * cc]) = f32x4{acc[0], acc[1], acc[2], acc[3]};
      *reinterpret_cast<f32x4*>(&lds[L_RED + 576 * r0 + 8 * cc + 4]) = f32x4{acc[4], acc[5], acc[6], acc[7]};
    }
  }
  __syncthreads();
  {
    gu64* g = ws + WS_GP + (size_t)b * D;
#pragma unroll
    for (int rep = 0; rep < 2; ++rep) {
      const int col = tid + 512 * rep;
      if (col < D) {
        float s = 0.f;
#pragma unroll
        for (int r = 0; r < 7; ++r) s += lds[L_RED + 576 * r + col];
        st_gran(g + col, epoch, __builtin_bit_cast(u32, s));
      }
    }
  }

  if (b < NFIN) {
    const int col = 8 * b + (tid & 7);
    gu64* g = ws + WS_GP + (size_t)(tid >> 3) * D + col;
    u64 a[4];
    for (unsigned spins = 0;; ++spins) {
      bool ok = true;
#pragma unroll
      for (int i = 0; i < 4; ++i) { a[i] = ld_gran(g + (size_t)(64 * i) * D); ok &= (u32)(a[i] >> 32) == epoch; }
      if (__all(ok) || spins > SPIN_CAP) break;
      __builtin_amdgcn_s_sleep(1);
    }
    float sp = __builtin_bit_cast(float, (u32)a[0]) + __builtin_bit_cast(float, (u32)a[1]);
    float dp = __builtin_bit_cast(float, (u32)a[2]) + __builtin_bit_cast(float, (u32)a[3]);
    sp += dpp<XOR8>(sp); sp = xrow_sum(sp);
    dp += dpp<XOR8>(dp); dp = xrow_sum(dp);
    if (lane < 8) { lds[L_FIN + 16 * wave + lane] = sp; lds[L_FIN + 16 * wave + 8 + lane] = dp; }
    __syncthreads();
    if (tid < 8) {
      float s = 0.f, d = 0.f;
#pragma unroll
      for (int w = 0; w < 8; ++w) { s += lds[L_FIN + 16 * w + tid]; d += lds[L_FIN + 16 * w + 8 + tid]; }
      const float sb = bfr(s), db = bfr(d);
      const float gate = bfr(silu_f(db));
      const float prod = bfr(sb * gate);
      const float xo = bf2f(x[8 * b + tid]);
      out[8 * b + tid] = (u16)f2bf(xo + prod);
    }
    if (b == 0 && tid == 0) __hip_atomic_store((gu32*)(ws + WS_CNT), epoch, RLX_AGENT);
  }
}

}

extern "C" void kernel_launch(void* const* d_in, const int* in_sizes, int n_in,
                              void* d_out, int out_size, void* d_ws, size_t ws_size,
                              hipStream_t stream) {
  const u16* x = (const u16*)d_in[0];
  const u16* kc = (const u16*)d_in[1];
  const u16* vc = (const u16*)d_in[2];
  const u16* wq = (const u16*)d_in[3];
  const u16* wo = (const u16*)d_in[4];
  const u16* wg = (const u16*)d_in[6];
  const u16* wu = (const u16*)d_in[7];
  const u16* wd = (const u16*)d_in[8];
  hipLaunchKernelGGL(fused_block_kernel, dim3(NWG), dim3(NTHR), 0, stream,
                     x, kc, vc, wq, wo, wg, wu, wd, (u16*)d_out, (u64*)d_ws);
}
```

```cpp
#include <hip/hip_runtime.h>
#include <hip/hip_bf16.h>
#include <stdint.h>

typedef unsigned int u32;
typedef unsigned long long u64;
typedef unsigned short u16;
typedef __attribute__((ext_vector_type(4))) unsigned u32x4;
typedef __attribute__((ext_vector_type(4))) float f32x4;
typedef __attribute__((ext_vector_type(2))) __bf16 bf16x2;
#define GAS __attribute__((address_space(1)))
typedef GAS u64 gu64;
typedef GAS u32 gu32;
#define RLX_AGENT __ATOMIC_RELAXED, __HIP_MEMORY_SCOPE_AGENT

namespace {

constexpr int D = 576, HKV = 16, SK = 16384, DH = 128, WIN = 4096, FF = 4608, QC = 4096;
constexpr int NWG = 256, NTHR = 512;
constexpr int NTILE_A = 208;
constexpr int NSPLIT = 16;
constexpr int OREC = 136;
constexpr int NFIN = 72;

constexpr size_t WS_CNT = 0;
constexpr size_t WS_GQ = 32;
constexpr size_t WS_GG = WS_GQ + QC / 2;
constexpr size_t WS_GU = WS_GG + FF / 2;
constexpr size_t WS_GO = WS_GU + FF / 2;
constexpr size_t WS_GP = WS_GO + 32 * NSPLIT * OREC;
constexpr size_t WS_END = WS_GP + (size_t)NWG * D;

constexpr unsigned SPIN_CAP = 1u << 20;
#define STAMP(k) do { } while (0)

__device__ __forceinline__ float bf2f(u16 v) { return __builtin_bit_cast(float, (u32)v << 16); }
__device__ __forceinline__ float bflo(u32 v) { return __builtin_bit_cast(float, v << 16); }
__device__ __forceinline__ float bfhi(u32 v) { return __builtin_bit_cast(float, v & 0xffff0000u); }
__device__ __forceinline__ u32 f2bf(float f) {
  u32 u = __builtin_bit_cast(u32, f);
  return (u + 0x7fffu + ((u >> 16) & 1u)) >> 16;
}
__device__ __forceinline__ float bfr(float f) { return __builtin_bit_cast(float, f2bf(f) << 16); }
__device__ __forceinline__ float dot2(u32 a, u32 b, float acc) {
  return __builtin_amdgcn_fdot2_f32_bf16(__builtin_bit_cast(bf16x2, a), __builtin_bit_cast(bf16x2, b), acc, false);
}
template <int CTRL> __device__ __forceinline__ float dpp(float x) {
  return __builtin_bit_cast(float, __builtin_amdgcn_mov_dpp(__builtin_bit_cast(int, x), CTRL, 0xf, 0xf, true));
}
constexpr int XOR1 = 0xB1, XOR2 = 0x4E, XOR7 = 0x141, XOR8 = 0x128, ROR4 = 0x124;
__device__ __forceinline__ float xrow_max(float x) {
  auto s = __builtin_amdgcn_permlane16_swap(__float_as_uint(x), __float_as_uint(x), false, false);
  x = fmaxf(__uint_as_float(s[0]), __uint_as_float(s[1]));
  auto t = __builtin_amdgcn_permlane32_swap(__float_as_uint(x), __float_as_uint(x), false, false);
  return fmaxf(__uint_as_float(t[0]), __uint_as_float(t[1]));
}
__device__ __forceinline__ float xrow_sum(float x) {
  auto s = __builtin_amdgcn_permlane16_swap(__float_as_uint(x), __float_as_uint(x), false, false);
  x = __uint_as_float(s[0]) + __uint_as_float(s[1]);
  auto t = __builtin_amdgcn_permlane32_swap(__float_as_uint(x), __float_as_uint(x), false, false);
  return __uint_as_float(t[0]) + __uint_as_float(t[1]);
}
__device__ __forceinline__ float wave_sum(float x) {
  x += dpp<XOR1>(x); x += dpp<XOR2>(x); x += dpp<ROR4>(x); x += dpp<XOR8>(x);
  return xrow_sum(x);
}
__device__ __forceinline__ void st_gran(gu64* p, u32 tag, u32 val) {
  __hip_atomic_store(p, ((u64)tag << 32) | (u64)val, RLX_AGENT);
}
__device__ __forceinline__ u64 ld_gran(gu64* p) { return __hip_atomic_load(p, RLX_AGENT); }
__device__ __forceinline__ u32x4 ld_stream(const void* p) { return __builtin_nontemporal_load(reinterpret_cast<const u32x4*>(p)); }
__device__ __forceinline__ float silu_f(float v) { return v / (1.0f + __expf(-v)); }

constexpr int L_PART = 0;
constexpr int L_QS = 512;
constexpr int L_P = 640;
constexpr int L_MRG = L_P + 512;
constexpr int L_OS = L_MRG + 8 * 2 * 132;
constexpr int L_VAL = L_OS + 16 * 36;
constexpr int L_RED = L_VAL + 40;
constexpr int L_FIN = L_RED + 7 * 576;
constexpr int L_TOTAL = L_FIN + 128;

__global__ void __launch_bounds__(NTHR, 2)
fused_block_kernel(const u16* __restrict__ x, const u16* __restrict__ kc, const u16* __restrict__ vc,
                   const u16* __restrict__ wq, const u16* __restrict__ wo, const u16* __restrict__ wg,
                   const u16* __restrict__ wu, const u16* __restrict__ wd, u16* __restrict__ out,
                   u64* wsraw) {
  __shared__ __attribute__((aligned(16))) float lds[L_TOTAL];
  gu64* ws = (gu64*)wsraw;
  const int b = blockIdx.x, tid = threadIdx.x, lane = tid & 63;
  const int wave = __builtin_amdgcn_readfirstlane(tid >> 6);

  STAMP(0);
  const u32 epoch = (u32)__hip_atomic_load((gu32*)(ws + WS_CNT), RLX_AGENT) + 1u;

  float xs[9];
#pragma unroll
  for (int i = 0; i < 9; ++i) xs[i] = bf2f(x[lane + 64 * i]);
  const bool hasA = b < NTILE_A;
  float xr[9];
#pragma unroll
  for (int i = 0; i < 9; ++i) xr[i] = bf2f(x[72 * wave + 8 * i + (lane >> 3)]);

  const u16* wa; int ncolsA, col0; size_t dstA;
  if (b < 64) { wa = wq; ncolsA = QC; col0 = 64 * b; dstA = WS_GQ + 32 * b; }
  else if (b < 136) { wa = wg; ncolsA = FF; col0 = 64 * (b - 64); dstA = WS_GG + 32 * (b - 64); }
  else { wa = wu; ncolsA = FF; col0 = 64 * (b - 136); dstA = WS_GU + 32 * (b - 136); }
  u32x4 wA[9];
  const bool earlyA = hasA, lateA = false;
  const u16* pa = wa + (size_t)(72 * wave + (lane >> 3)) * ncolsA + col0 + 8 * (lane & 7);
#define ISSUE_A() do { _Pragma("unroll") for (int i = 0; i < 9; ++i) wA[i] = ld_stream(pa + (size_t)(8 * i) * ncolsA); } while (0)
  if (earlyA) {
    ISSUE_A();
    __builtin_amdgcn_s_barrier();
  }
  const int kvh = b >> 4, spl = b & 15;
  const size_t kvoff = ((size_t)kvh * SK + (SK - WIN) + 256 * spl + 32 * wave) * DH + 8 * lane;
  u32x4 Kr[8], Vr[8];
#pragma unroll
  for (int i = 0; i < 8; ++i) Kr[i] = ld_stream(kc + kvoff + 512 * i);

  const bool isWo = b < 128;
  const int nrowsC = isWo ? 32 : 36;
  const u16* wc = isWo ? (wo + (size_t)(32 * b) * D) : (wd + (size_t)(36 * (b - 128)) * D);
  const int r0 = tid / 72, cc = tid - 72 * r0;
  u32x4 wC[6];
#define ISSUE_VC() do { \
    _Pragma("unroll") for (int i = 0; i < 8; ++i) Vr[i] = ld_stream(vc + kvoff + 512 * i); \
    if (lateA) ISSUE_A(); \
    _Pragma("unroll") for (int i = 0; i < 6; ++i) { const int row = 7 * i + r0; wC[i] = u32x4{0u, 0u, 0u, 0u}; \
      if (tid < 504 && row < nrowsC) wC[i] = ld_stream(wc + (size_t)(504 * i + tid) * 8); } } while (0)

  float sum = 0.f;
#pragma unroll
  for (int i = 0; i < 9; ++i) sum += xs[i];
  const float mean = wave_sum(sum) * (1.0f / D);
  float sq = 0.f;
#pragma unroll
  for (int i = 0; i < 9; ++i) { const float d = xs[i] - mean; sq += d * d; }
  const float var = wave_sum(sq) * (1.0f / D);
  const float m_bf = bfr(mean), v_bf = bfr(var);
  const float den = bfr(sqrtf(bfr(v_bf + 1e-5f)));

  auto phaseA = [&]() {
    float acc[8];
#pragma unroll
    for (int k = 0; k < 8; ++k) acc[k] = 0.f;
#pragma unroll
    for (int i = 0; i < 9; ++i) {
      const float xn = bfr(bfr(xr[i] - m_bf) / den);
#pragma unroll
      for (int k = 0; k < 4; ++k) { acc[2 * k] = fmaf(xn, bflo(wA[i][k]), acc[2 * k]); acc[2 * k + 1] = fmaf(xn, bfhi(wA[i][k]), acc[2 * k + 1]); }
    }
#pragma unroll
    for (int k = 0; k < 8; ++k) { acc[k] += dpp<XOR8>(acc[k]); acc[k] = xrow_sum(acc[k]); }
    if (lane < 8) {
      *reinterpret_cast<f32x4*>(&lds[L_PART + 64 * wave + 8 * lane]) = f32x4{acc[0], acc[1], acc[2], acc[3]};
      *reinterpret_cast<f32x4*>(&lds[L_PART + 64 * wave + 8 * lane + 4]) = f32x4{acc[4], acc[5], acc[6], acc[7]};
    }
    __syncthreads();
    if (tid < 32) {
      float s0 = 0.f, s1 = 0.f;
#pragma unroll
      for (int w = 0; w < 8; ++w) { s0 += lds[L_PART + 64 * w + 2 * tid]; s1 += lds[L_PART + 64 * w + 2 * tid + 1]; }
      st_gran(ws + dstA + tid, epoch, f2bf(s0) | (f2bf(s1) << 16));
    }
  };
  if (earlyA) phaseA();
  STAMP(1);

  if (wave == 0) {
    gu64* g = ws + WS_GQ + 128 * kvh + lane;
    u64 a0, a1;
    for (unsigned spins = 0;; ++spins) {
      a0 = ld_gran(g); a1 = ld_gran(g + 64);
      const bool ok = (u32)(a0 >> 32) == epoch && (u32)(a1 >> 32) == epoch;
      if (__all(ok) || spins > SPIN_CAP) break;
      __builtin_amdgcn_s_sleep(1);
    }
    reinterpret_cast<u32*>(lds + L_QS)[lane] = (u32)a0;
    reinterpret_cast<u32*>(lds + L_QS)[64 + lane] = (u32)a1;
  }
  ISSUE_VC();
  __syncthreads();
  STAMP(2);

  {
    const int r = lane >> 4, c = lane & 15;
    const u32x4 q0 = *reinterpret_cast<const u32x4*>(lds + L_QS + 4 * c);
    const u32x4 q1 = *reinterpret_cast<const u32x4*>(lds + L_QS + 64 + 4 * c);
    float xv[16];
#pragma unroll
    for (int i = 0; i < 8; ++i) {
      float d0 = 0.f, d1 = 0.f;
#pragma unroll
      for (int k = 0; k < 4; ++k) { d0 = dot2(Kr[i][k], q0[k], d0); d1 = dot2(Kr[i][k], q1[k], d1); }
      xv[2 * i] = d0; xv[2 * i + 1] = d1;
    }
#define TR_STEP(HALF, CTRL, BIT) _Pragma("unroll") for (int h = 0; h < (HALF); ++h) { \
      const float lo_ = xv[h], hi_ = xv[h + (HALF)]; const float t1_ = lo_ + dpp<CTRL>(lo_), t2_ = hi_ + dpp<CTRL>(hi_); \
      xv[h] = (c & (BIT)) ? t2_ : t1_; }
    TR_STEP(8, XOR8, 8) TR_STEP(4, XOR7, 4) TR_STEP(2, XOR2, 2) TR_STEP(1, XOR1, 1)
#undef TR_STEP
    const float sc = bfr(bfr(xv[0]) * 0.08838834764831845f);
    float mx = sc;
    mx = fmaxf(mx, dpp<XOR2>(mx)); mx = fmaxf(mx, dpp<ROR4>(mx)); mx = fmaxf(mx, dpp<XOR8>(mx)); mx = xrow_max(mx);
    const float p = __expf(sc - mx);
    float ls = p;
    ls += dpp<XOR2>(ls); ls += dpp<ROR4>(ls); ls += dpp<XOR8>(ls); ls = xrow_sum(ls);
    float* prow = lds + L_P + 64 * wave + 16 * r;
    prow[c] = p;
    asm volatile("s_waitcnt lgkmcnt(0)" ::: "memory");
    f32x4 pr[4];
#pragma unroll
    for (int u = 0; u < 4; ++u) pr[u] = *reinterpret_cast<const f32x4*>(prow + 4 * u);
    float o0[8], o1[8];
#pragma unroll
    for (int k = 0; k < 8; ++k) { o0[k] = 0.f; o1[k] = 0.f; }
#pragma unroll
    for (int i = 0; i < 8; ++i) {
      const float p0 = pr[i >> 1][(2 * i) & 3], p1 = pr[i >> 1][(2 * i + 1) & 3];
#pragma unroll
      for (int k = 0; k < 4; ++k) {
        const float vl = bflo(Vr[i][k]), vh = bfhi(Vr[i][k]);
        o0[2 * k] = fmaf(p0, vl, o0[2 * k]); o0[2 * k + 1] = fmaf(p0, vh, o0[2 * k + 1]);
        o1[2 * k] = fmaf(p1, vl, o1[2 * k]); o1[2 * k + 1] = fmaf(p1, vh, o1[2 * k + 1]);
      }
    }
#pragma unroll
    for (int k = 0; k < 8; ++k) { o0[k] = xrow_sum(o0[k]); o1[k] = xrow_sum(o1[k]); }
    float* rec0 = lds + L_MRG + (wave * 2 + 0) * 132;
    float* rec1 = lds + L_MRG + (wave * 2 + 1) * 132;
    if (r == 0) {
      *reinterpret_cast<f32x4*>(rec0 + 4 + 8 * c) = f32x4{o0[0], o0[1], o0[2], o0[3]};
      *reinterpret_cast<f32x4*>(rec0 + 8 + 8 * c) = f32x4{o0[4], o0[5], o0[6], o0[7]};
      *reinterpret_cast<f32x4*>(rec1 + 4 + 8 * c) = f32x4{o1[0], o1[1], o1[2], o1[3]};
      *reinterpret_cast<f32x4*>(rec1 + 8 + 8 * c) = f32x4{o1[4], o1[5], o1[6], o1[7]};
      if (c < 2) { float* rec = c ? rec1 : rec0; rec[0] = mx; rec[1] = ls; }
    }
  }
  __syncthreads();
  if (tid < 256) {
    const int h = tid >> 7, d = tid & 127;
    float M = -INFINITY;
#pragma unroll
    for (int w = 0; w < 8; ++w) M = fmaxf(M, lds[L_MRG + (w * 2 + h) * 132]);
    float L = 0.f, o = 0.f;
#pragma unroll
    for (int w = 0; w < 8; ++w) {
      const float* rec = lds + L_MRG + (w * 2 + h) * 132;
      const float wt = __expf(rec[0] - M);
      L = fmaf(wt, rec[1], L); o = fmaf(wt, rec[4 + d], o);
    }
    gu64* g = ws + WS_GO + (size_t)((2 * kvh + h) * NSPLIT + spl) * OREC;
    st_gran(g + d, epoch, __builtin_bit_cast(u32, o));
    if (d < 2) st_gran(g + 128 + d, epoch, __builtin_bit_cast(u32, d ? L : M));
  }

  if (lateA) phaseA();
  STAMP(3);
  if (isWo) {
    const int h = b >> 2, dq = b & 3;
    const int s = 2 * wave + (lane >> 5);
    gu64* g = ws + WS_GO + (size_t)(h * NSPLIT + s) * OREC;
    u64 a0, a1;
    for (unsigned spins = 0;; ++spins) {
      a0 = ld_gran(g + 32 * dq + (lane & 31));
      a1 = ld_gran(g + 128 + (lane & 1));
      const bool ok = (u32)(a0 >> 32) == epoch && (u32)(a1 >> 32) == epoch;
      if (__all(ok) || spins > SPIN_CAP) break;
      __builtin_amdgcn_s_sleep(1);
    }
    lds[L_OS + 36 * s + (lane & 31)] = __builtin_bit_cast(float, (u32)a0);
    if ((lane & 31) < 2) lds[L_OS + 36 * s + 32 + (lane & 1)] = __builtin_bit_cast(float, (u32)a1);
  } else if (wave == 0) {
    const int cidx = b - 128;
    gu64* g = ws + ((lane < 32) ? WS_GG : WS_GU) + 18 * cidx + (lane & 31);
    const bool act = (lane & 31) < 18;
    u64 a0 = 0;
    for (unsigned spins = 0;; ++spins) {
      if (act) a0 = ld_gran(g);
      const bool ok = !act || (u32)(a0 >> 32) == epoch;
      if (__all(ok) || spins > SPIN_CAP) break;
      __builtin_amdgcn_s_sleep(1);
    }
    const u32 gp = (u32)a0;
    const u32 up = (u32)__shfl((int)gp, lane + 32);
    if (lane < 18) {
      const float g0 = bflo(gp), g1 = bfhi(gp), u0 = bflo(up), u1 = bfhi(up);
      lds[L_VAL + 2 * lane] = bfr(bfr(silu_f(g0)) * u0);
      lds[L_VAL + 2 * lane + 1] = bfr(bfr(silu_f(g1)) * u1);
    }
  }
  __syncthreads();
  if (isWo && tid < 32) {
    float M = -INFINITY;
#pragma unroll
    for (int s = 0; s < NSPLIT; ++s) M = fmaxf(M, lds[L_OS + 36 * s + 32]);
    float L = 0.f, o = 0.f;
#pragma unroll
    for (int s = 0; s < NSPLIT; ++s) {
      const float wt = __expf(lds[L_OS + 36 * s + 32] - M);
      L = fmaf(wt, lds[L_OS + 36 * s + 33], L); o = fmaf(wt, lds[L_OS + 36 * s + tid], o);
    }
    lds[L_VAL + tid] = bfr(o / L);
  }
  __syncthreads();

  STAMP(4);
  {
    float acc[8];
#pragma unroll
    for (int k = 0; k < 8; ++k) acc[k] = 0.f;
#pragma unroll
    for (int i = 0; i < 6; ++i) {
      const int row = 7 * i + r0;
      if (tid < 504 && row < nrowsC) {
        const float v = lds[L_VAL + row];
#pragma unroll
        for (int k = 0; k < 4; ++k) { acc[2 * k] = fmaf(v, bflo(wC[i][k]), acc[2 * k]); acc[2 * k + 1] = fmaf(v, bfhi(wC[i][k]), acc[2 * k + 1]); }
      }
    }
    if (tid < 504) {
      *reinterpret_cast<f32x4*>(&lds[L_RED + 576 * r0 + 8 * cc]) = f32x4{acc[0], acc[1], acc[2], acc[3]};
      *reinterpret_cast<f32x4*>(&lds[L_RED + 576 * r0 + 8 * cc + 4]) = f32x4{acc[4], acc[5], acc[6], acc[7]};
    }
  }
  __syncthreads();
  {
    gu64* g = ws + WS_GP + (size_t)b * D;
#pragma unroll
    for (int rep = 0; rep < 2; ++rep) {
      const int col = tid + 512 * rep;
      if (col < D) {
        float s = 0.f;
#pragma unroll
        for (int r = 0; r < 7; ++r) s += lds[L_RED + 576 * r + col];
        st_gran(g + col, epoch, __builtin_bit_cast(u32, s));
      }
    }
  }

  STAMP(5);
  if (b < NFIN) {
    const int col = 8 * b + (tid & 7);
    gu64* g = ws + WS_GP + (size_t)(tid >> 3) * D + col;
    u64 a[4];
    for (unsigned spins = 0;; ++spins) {
      bool ok = true;
#pragma unroll
      for (int i = 0; i < 4; ++i) { a[i] = ld_gran(g + (size_t)(64 * i) * D); ok &= (u32)(a[i] >> 32) == epoch; }
      if (__all(ok) || spins > SPIN_CAP) break;
      __builtin_amdgcn_s_sleep(1);
    }
    STAMP(6);
    float sp = __builtin_bit_cast(float, (u32)a[0]) + __builtin_bit_cast(float, (u32)a[1]);
    float dp = __builtin_bit_cast(float, (u32)a[2]) + __builtin_bit_cast(float, (u32)a[3]);
    sp += dpp<XOR8>(sp); sp = xrow_sum(sp);
    dp += dpp<XOR8>(dp); dp = xrow_sum(dp);
    if (lane < 8) { lds[L_FIN + 16 * wave + lane] = sp; lds[L_FIN + 16 * wave + 8 + lane] = dp; }
    __syncthreads();
    if (tid < 8) {
      float s = 0.f, d = 0.f;
#pragma unroll
      for (int w = 0; w < 8; ++w) { s += lds[L_FIN + 16 * w + tid]; d += lds[L_FIN + 16 * w + 8 + tid]; }
      const float sb = bfr(s), db = bfr(d);
      const float gate = bfr(silu_f(db));
      const float prod = bfr(sb * gate);
      const float xo = bf2f(x[8 * b + tid]);
      out[8 * b + tid] = (u16)f2bf(xo + prod);
    }
    if (b == 0 && tid == 0) __hip_atomic_store((gu32*)(ws + WS_CNT), epoch, RLX_AGENT);
  }
}

}

extern "C" void kernel_launch(void* const* d_in, const int* in_sizes, int n_in,
                              void* d_out, int out_size, void* d_ws, size_t ws_size,
                              hipStream_t stream) {
  const u16* x = (const u16*)d_in[0];
  const u16* kc = (const u16*)d_in[1];
  const u16* vc = (const u16*)d_in[2];
  const u16* wq = (const u16*)d_in[3];
  const u16* wo = (const u16*)d_in[4];
  const u16* wg = (const u16*)d_in[6];
  const u16* wu = (const u16*)d_in[7];
  const u16* wd = (const u16*)d_in[8];
  hipLaunchKernelGGL(fused_block_kernel, dim3(NWG), dim3(NTHR), 0, stream,
                     x, kc, vc, wq, wo, wg, wu, wd, (u16*)d_out, (u64*)d_ws);
}
```
